```python
import torch

def _softmax(x, axis=-1):
    return torch.softmax(x, dim=axis)
def _gelu(x):
    return torch.nn.functional.gelu(x, approximate="tanh")
def _silu(x):
    return torch.nn.functional.silu(x)
def _sigmoid(x):
    return torch.sigmoid(x)
def _layernorm(x, axis=-1, eps=1e-5):
    m = x.mean(dim=axis, keepdim=True)
    v = x.var(dim=axis, keepdim=True, unbiased=False)
    return (x - m) / torch.sqrt(v + eps)
def _scatter_add(tgt, idx, src, axis=0):
    return tgt.clone().index_add_(axis, idx, src)
def _segment_sum(src, seg, n):
    out = torch.zeros((n,) + src.shape[1:], dtype=src.dtype)
    return out.index_add_(0, seg, src)
def _pad(x, pw):
    flat = tuple(v for pair in reversed(pw) for v in pair)
    return torch.nn.functional.pad(x, flat)

kernel_name = 'interleave_layers__sliding_block_cached__dense_activation_gelu__decode'

def setup_inputs():

    _w = (lambda a: (lambda t: t.to(torch.bfloat16) if t.is_floating_point() else t)(torch.as_tensor(a)))
    def _A_setup():
        B, Sq, D = 4, 1, 576
        Hq, Hkv, Sk, Dh = 32, 16, 8192, 128
        assert Hq % Hkv == 0, (Hq, Hkv)
        return {
            "x": _w((lambda *s: torch.randn(*s, dtype=torch.bfloat16) * 0.05)(B, Sq, D)),
            "k_cache": _w((lambda *s: torch.randn(*s, dtype=torch.bfloat16) * 0.8)(B, Hkv, Sk, Dh)),
            "v_cache": _w((lambda *s: torch.randn(*s, dtype=torch.bfloat16) * 1.6)(B, Hkv, Sk, Dh)),
            "wq": _w((lambda *s: torch.randn(*s, dtype=torch.bfloat16) * 0.8)(D, Hq * Dh) * (D ** -0.5)),
            "wo": _w((lambda *s: torch.randn(*s, dtype=torch.bfloat16) * 1.6)(Hq * Dh, D) * ((Hq * Dh) ** -0.5)),
        }
    _A_in = _A_setup()

    def _B_setup():
        B, S, D, F = 4, 1, 576, 4608
        return {
            "x": _w((lambda *s: torch.randn(*s, dtype=torch.bfloat16) * 0.05)(B, S, D)),
            "w1": _w((lambda *s: torch.randn(*s, dtype=torch.bfloat16) * 0.4)(D, F) * (D ** -0.5)),
            "b1": _w((lambda *s: torch.randn(*s, dtype=torch.bfloat16) * 0.4)(F) * 0.01),
            "w2": _w((lambda *s: torch.randn(*s, dtype=torch.bfloat16) * 0.4)(F, D) * (F ** -0.5)),
            "b2": _w((lambda *s: torch.randn(*s, dtype=torch.bfloat16) * 0.4)(D) * 0.01),
        }
    _B_in = _B_setup()

    return {
        "A_x": _A_in["x"],
        "A_k_cache": _A_in["k_cache"],
        "A_v_cache": _A_in["v_cache"],
        "A_wq": _A_in["wq"],
        "A_wo": _A_in["wo"],
        "B_x": _B_in["x"],
        "B_w1": _B_in["w1"],
        "B_b1": _B_in["b1"],
        "B_w2": _B_in["w2"],
        "B_b2": _B_in["b2"],
    }

def reference(A_x, A_k_cache, A_v_cache, A_wq, A_wo, B_x, B_w1, B_b1, B_w2, B_b2):
    def _A_ref(x, k_cache, v_cache, wq, wo):
        B, Sq, D = x.shape
        Hq, Dh = 32, 128
        Hkv, Sk = k_cache.shape[1], k_cache.shape[2]
        W = min(4096, max(256, Sk // 4))
        rep = Hq // Hkv
        q = (lambda x, *d: x.permute(*d))((x @ wq).reshape(B, Sq, Hq, Dh), 0, 2, 1, 3)
        k = (lambda x, n, axis: torch.repeat_interleave(x, n, dim=axis))(k_cache[:, :, Sk - W:, :], rep, axis=1)
        v = (lambda x, n, axis: torch.repeat_interleave(x, n, dim=axis))(v_cache[:, :, Sk - W:, :], rep, axis=1)
        scores = torch.einsum("bhqd,bhkd->bhqk", q, k) * (Dh ** -0.5)
        p = _softmax(scores, axis=-1)
        o = torch.einsum("bhqk,bhkd->bhqd", p, v)
        return (lambda x, *d: x.permute(*d))(o, 0, 2, 1, 3).reshape(B, Sq, Hq * Dh) @ wo

    def _B_ref(x, w1, b1, w2, b2):
        h = torch.einsum("bsd,df->bsf", x, w1) + b1
        h = _gelu(h)
        return torch.einsum("bsf,fd->bsd", h, w2) + b2

    x = A_x
    x = x + _A_ref(_layernorm(x, axis=-1), A_k_cache, A_v_cache, A_wq, A_wo)
    x = x + _B_ref(_layernorm(x, axis=-1), B_w1, B_b1, B_w2, B_b2)
    x = x + _A_ref(_layernorm(x, axis=-1), A_k_cache, A_v_cache, A_wq, A_wo)
    x = x + _B_ref(_layernorm(x, axis=-1), B_w1, B_b1, B_w2, B_b2)
    return x
```

```cpp
#include <hip/hip_runtime.h>
#include <hip/hip_bf16.h>
#include <stdint.h>
typedef unsigned short u16;
__device__ __forceinline__ float bf2f(u16 b){ return __uint_as_float(((unsigned)b)<<16); }
__device__ __forceinline__ u16 f2bf(float f){ unsigned u=__float_as_uint(f); u += 0x7fff + ((u>>16)&1); return (u16)(u>>16); }
constexpr int B=4,D=576,HQ=32,HKV=16,SK=8192,DH=128,W=2048,F=4608;

__global__ void k_init(const u16* x, float* xf){ int i=blockIdx.x*256+threadIdx.x; if(i<B*D) xf[i]=bf2f(x[i]); }
template<int MODE>
__global__ void k_ln_gemv(const float* xf, const u16* Wt, const u16* bias, float* y, int N){
  __shared__ float ln[B][D];
  __shared__ float red[2][B];
  int t=threadIdx.x;
  if(t<B*64){ int b=t>>6,l=t&63; float s=0; for(int k=l;k<D;k+=64) s+=xf[b*D+k];
    for(int o=32;o;o>>=1) s+=__shfl_xor(s,o); float m=s/D; float v=0; for(int k=l;k<D;k+=64){float d=xf[b*D+k]-m; v+=d*d;}
    for(int o=32;o;o>>=1) v+=__shfl_xor(v,o); v/=D; float r=1.0f/sqrtf(v+1e-5f);
    for(int k=l;k<D;k+=64) ln[b][k]=(xf[b*D+k]-m)*r; }
  __syncthreads();
  int n=blockIdx.x*256+t; if(n>=N) return;
  float a[B]={0,0,0,0};
  for(int k=0;k<D;k++){ float w=bf2f(Wt[(size_t)k*N+n]); for(int b=0;b<B;b++) a[b]+=ln[b][k]*w; }
  for(int b=0;b<B;b++){ float v=a[b]; if(MODE==1){ v+=bf2f(bias[n]); float u=0.7978845608028654f*(v+0.044715f*v*v*v); v=0.5f*v*(1.0f+tanhf(u)); } y[b*N+n]=v; }
}
__global__ void k_attn(const float* q, const u16* kc, const u16* vc, float* o){
  int b=blockIdx.x/HQ,h=blockIdx.x%HQ,g=h/2; __shared__ float s[W]; __shared__ float qs[DH]; __shared__ float red[256];
  int t=threadIdx.x; if(t<DH) qs[t]=q[b*HQ*DH+h*DH+t]; __syncthreads();
  const u16* K=kc+((size_t)(b*HKV+g)*SK+(SK-W))*DH; const u16* V=vc+((size_t)(b*HKV+g)*SK+(SK-W))*DH;
  float mx=-1e30f; for(int j=t;j<W;j+=256){ float a=0; for(int d=0;d<DH;d++) a+=qs[d]*bf2f(K[(size_t)j*DH+d]); a*=0.08838834764831845f; s[j]=a; mx=fmaxf(mx,a);}
  red[t]=mx; __syncthreads(); for(int o2=128;o2;o2>>=1){ if(t<o2) red[t]=fmaxf(red[t],red[t+o2]); __syncthreads(); } mx=red[0]; __syncthreads();
  float sum=0; for(int j=t;j<W;j+=256){ float p=expf(s[j]-mx); s[j]=p; sum+=p;} red[t]=sum; __syncthreads(); for(int o2=128;o2;o2>>=1){ if(t<o2) red[t]+=red[t+o2]; __syncthreads(); } sum=red[0];
  if(t<DH){ float a=0; for(int j=0;j<W;j++) a+=s[j]*bf2f(V[(size_t)j*DH+t]); o[b*HQ*DH+h*DH+t]=a/sum; }
}
__global__ void k_gemv_res(const float* a, const u16* Wt, const u16* bias, float* xf, int K, u16* out){
  int n=blockIdx.x; int t=threadIdx.x; __shared__ float red[B][256]; float acc[B]={0,0,0,0};
  for(int k=t;k<K;k+=256){ float w=bf2f(Wt[(size_t)k*D+n]); for(int b=0;b<B;b++) acc[b]+=a[b*K+k]*w; }
  for(int b=0;b<B;b++) red[b][t]=acc[b]; __syncthreads();
  for(int o=128;o;o>>=1){ if(t<o) for(int b=0;b<B;b++) red[b][t]+=red[b][t+o]; __syncthreads(); }
  if(t<B){ float v=xf[t*D+n]+red[t][0]+(bias?bf2f(bias[n]):0.f); u16 r=f2bf(v); xf[t*D+n]=bf2f(r); if(out) out[t*D+n]=r; }
}
extern "C" void kernel_launch(void* const* d_in, const int* in_sizes, int n_in, void* d_out, int out_size, void* d_ws, size_t ws_size, hipStream_t stream){
  const u16* x=(const u16*)d_in[0]; const u16* kc=(const u16*)d_in[1]; const u16* vc=(const u16*)d_in[2]; const u16* wq=(const u16*)d_in[3]; const u16* wo=(const u16*)d_in[4];
  const u16* w1=(const u16*)d_in[6]; const u16* b1=(const u16*)d_in[7]; const u16* w2=(const u16*)d_in[8]; const u16* b2=(const u16*)d_in[9];
  float* xf=(float*)d_ws; float* q=xf+4096; float* o=q+B*4096; float* h=o+B*4096;
  k_init<<<9,256,0,stream>>>(x,xf);
  for(int L=0;L<2;L++){
    k_ln_gemv<0><<<16,256,0,stream>>>(xf,wq,nullptr,q,4096);
    k_attn<<<B*HQ,256,0,stream>>>(q,kc,vc,o);
    k_gemv_res<<<D,256,0,stream>>>(o,wo,nullptr,xf,4096,nullptr);
    k_ln_gemv<1><<<18,256,0,stream>>>(xf,w1,b1,h,F);
    k_gemv_res<<<D,256,0,stream>>>(h,w2,b2,xf,F,L==1?(u16*)d_out:nullptr);
  }
}
```

```cpp
#define SCHED_TAB 30,245,331,453,543,683,773,851,957
#define CODE_KB 34
#define TOUCH 1
#include <hip/hip_runtime.h>
#include <stdint.h>

typedef unsigned short u16;
typedef unsigned v4u __attribute__((ext_vector_type(4)));
typedef unsigned v3u __attribute__((ext_vector_type(3)));
typedef unsigned v2u __attribute__((ext_vector_type(2)));
typedef float f4 __attribute__((ext_vector_type(4)));
typedef float f2 __attribute__((ext_vector_type(2)));
typedef __bf16 bf8v __attribute__((ext_vector_type(8)));
typedef short s4v __attribute__((ext_vector_type(4)));
typedef unsigned v8u __attribute__((ext_vector_type(8)));
typedef __bf16 bf16v __attribute__((ext_vector_type(16)));
#define LAS __attribute__((address_space(3)))
typedef __amdgpu_buffer_rsrc_t rsrc_t;

#define STAMPS 0
#define ROT 5
#define HOLD 12
#define VPRE 11
#define VPREH 4
#define VQH 12
#define POLL_SLEEP 0
#define BT 7
#define BACKOFF 0
#define FIXO 0
#define T0FIRST 0
#define LITE 0
#define WBL 0
#define OUTAUX 0
#define GS 0
#define GWAIT 1
#define NREP 64
#define W2A 4
#define W2LEAD 100
#define CLR2 3
#define RSX 0
#define RSTOL 0
#define GOEARLY 1
#define HPRE 6
#define P0 3
#define VFIRST 0
#define VH1 2
#define SEQ 1
#define HL 8
#define RL1 1
#define NCAL 3
#define FLIP 0
#define XP 0
#define PL 3
#define HV 1
#define CPOS 1
#define T_F 450
#define KPRE 10
#define VPRE0 5
#define PRE_T 0
#define LATE_T 1050
#define G1 0
#define G2 1050
#define G3 1100
#define W2GAP 10
#define DUR 55
#define WSEND 1

namespace {
constexpr int ST_[13] = {SCHED_TAB};
constexpr int SB1 = ST_[0], SW_C1 = ST_[1], SW_P1 = ST_[2], SW_D1 = ST_[3], SW_A2 = ST_[4], SW_B2 = ST_[5], SW_C2 = ST_[6], SW_P2 = ST_[7], SW_D2 = ST_[8];
constexpr int PB_P1 = ST_[9], PB_A2 = ST_[10], PB_B2 = ST_[11], PB_P2 = ST_[12];
constexpr int D = 576, NQ = 4096, F = 4608, SK = 8192, WIN = 2048;
constexpr unsigned SENT = 0xFFFFFFFFu, ONE2 = 0x3F803F80u;
constexpr int AUX_SC1 = 16, AUX_NT = 2, OOB = 0x40000000;
constexpr float EPS = 1e-5f;
constexpr float SCL = 0.08838834764831845f * 1.4426950408889634f;
constexpr int A1S = 2688, BLK = 16384;
constexpr int WS_SIDE = 512;
constexpr int WS_CNT = 2048;
constexpr int WS_TCH = 4096;
constexpr int WS_A1 = 8192;
constexpr int WS_B1 = WS_A1 + 256 * A1S;
constexpr int WS_L1 = WS_B1 + 256 * 512;
constexpr int WS_P1 = WS_L1 + 256 * 128;
constexpr int WS_DBG = WS_P1 + 24 * 32768;
static_assert(WS_DBG + 65536 + 4096 <= (2 << 20), "first page");
constexpr int WS_S = 2 << 20;
constexpr int WS_CD = WS_S + 68 * BLK;
constexpr int WS_BCN = WS_S + 122 * BLK;
constexpr int WS_CAL = WS_S + 127 * BLK;
constexpr int WS_END = 4 << 20;
constexpr int L_WQ = 0, L_WO = 20480, L_W1 = L_WO + 32768, L_W2 = L_W1 + 24576;
constexpr int L_STG = L_WO;
constexpr int L_O = L_W2 + 24576;
constexpr int L_ML = L_O + 8192;
constexpr int L_Q = L_ML + 128;
constexpr int UXS = 192;
constexpr int SPR = 80, SPS = 8 * SPR;
constexpr int L_UX = L_Q + 1024;
constexpr int L_U1 = L_UX + 5 * UXS;
constexpr int L_UO = L_U1 + 10 * SPR;
constexpr int L_UH = L_UO + 4 * SPS;
constexpr int L_CST = L_UH + 3 * SPS;
constexpr int L_CS = L_CST + 1024;
constexpr int L_CNT = L_CS + 1024;
constexpr int L_QF = L_CNT + 48;
constexpr int L_GO = L_CNT + 64;
constexpr int L_DUM = L_CNT + 96;
constexpr int L_END = L_DUM + 256;
constexpr unsigned SPIN_CAP = 1u << 15, LDS_CAP = 1u << 22;

__device__ __forceinline__ float bf2f(unsigned b) { return __uint_as_float(b << 16); }
__device__ __forceinline__ float bfhi(unsigned b) { return __uint_as_float(b & 0xFFFF0000u); }
__device__ __forceinline__ unsigned cvtpk(float lo, float hi) {
  typedef __bf16 b2 __attribute__((ext_vector_type(2)));
  b2 r = {(__bf16)lo, (__bf16)hi};
  return __builtin_bit_cast(unsigned, r);
}
__device__ __forceinline__ float dot2(unsigned a, unsigned b, float c) {
  typedef __bf16 b2 __attribute__((ext_vector_type(2)));
  return __builtin_amdgcn_fdot2_f32_bf16(__builtin_bit_cast(b2, a), __builtin_bit_cast(b2, b), c, false);
}
__device__ __forceinline__ unsigned umax(unsigned a, unsigned b) { return a > b ? a : b; }
__device__ __forceinline__ unsigned umax4(unsigned m, v4u v) { return umax(umax(m, v.x), umax(umax(v.y, v.z), v.w)); }
__device__ __forceinline__ unsigned nosent(unsigned v) { return v < 0xFFFFFFFEu ? v : 0xFFFFFFFEu; }
__device__ __forceinline__ v2u pack4(f4 v) { return v2u{cvtpk(v.x, v.y), cvtpk(v.z, v.w)}; }
__device__ __forceinline__ f4 mfma(v4u a, v4u b, f4 c) {
  return __builtin_amdgcn_mfma_f32_16x16x32_bf16(__builtin_bit_cast(bf8v, a), __builtin_bit_cast(bf8v, b), c, 0, 0, 0);
}
__device__ __forceinline__ f4 smfma(v4u a, v8u b, f4 c, int idx) {
  return __builtin_amdgcn_smfmac_f32_16x16x64_bf16(__builtin_bit_cast(bf8v, a), __builtin_bit_cast(bf16v, b), c, idx, 0, 0);
}
__device__ __forceinline__ float vmax(float a, float b) { float r; asm("v_max_f32 %0, %1, %2" : "=v"(r) : "v"(a), "v"(b)); return r; }
__device__ __forceinline__ float par_max(float a) {
  float t;
  asm("s_nop 1\n\tv_max_f32_dpp %0, %0, %0 quad_perm:[2,3,0,1] row_mask:0xf bank_mask:0xf\n\ts_nop 1\n\t"
      "v_max_f32_dpp %0, %0, %0 row_ror:4 row_mask:0xf bank_mask:0xf\n\ts_nop 1\n\t"
      "v_max_f32_dpp %0, %0, %0 row_ror:8 row_mask:0xf bank_mask:0xf\n\t"
      "v_mov_b32 %1, %0\n\ts_nop 1\n\tv_permlane16_swap_b32 %0, %1\n\tv_max_f32 %0, %0, %1\n\t"
      "v_mov_b32 %1, %0\n\ts_nop 1\n\tv_permlane32_swap_b32 %0, %1\n\tv_max_f32 %0, %0, %1"
      : "+v"(a), "=&v"(t));
  return a;
}
__device__ __forceinline__ void xq_max2(float& a, float& b) {
  float t, u;
#define SM(op) "v_mov_b32 %2, %0\n\tv_mov_b32 %3, %1\n\ts_nop 0\n\t" op " %0, %2\n\t" op " %1, %3\n\tv_max_f32 %0, %0, %2\n\tv_max_f32 %1, %1, %3\n\t"
  asm("s_nop 0\n\t" SM("v_permlane16_swap_b32") SM("v_permlane32_swap_b32") "s_nop 0" : "+v"(a), "+v"(b), "=&v"(t), "=&v"(u));
#undef SM
}
template <int CTRL, int BANK> __device__ __forceinline__ unsigned dppb(unsigned old, unsigned x) { return (unsigned)__builtin_amdgcn_update_dpp((int)old, (int)x, CTRL, 0xf, BANK, false); }
__device__ __forceinline__ float sadd(float a, float b) { float r = a + b; asm("" : "+v"(r)); return r; }
template <int CTRL> __device__ __forceinline__ unsigned dppu(unsigned x) { return (unsigned)__builtin_amdgcn_mov_dpp((int)x, CTRL, 0xf, 0xf, true); }
template <int CTRL> __device__ __forceinline__ float dpp(float x) { return __uint_as_float(dppu<CTRL>(__float_as_uint(x))); }
constexpr int XOR1 = 0xB1, XOR2 = 0x4E, ROR4 = 0x124, ROR8 = 0x128;
__device__ __forceinline__ float xq_sum(float x) {
  auto s = __builtin_amdgcn_permlane16_swap(__float_as_uint(x), __float_as_uint(x), false, false);
  x = __uint_as_float(s[0]) + __uint_as_float(s[1]);
  auto t = __builtin_amdgcn_permlane32_swap(__float_as_uint(x), __float_as_uint(x), false, false);
  return __uint_as_float(t[0]) + __uint_as_float(t[1]);
}
__device__ __forceinline__ float xq_max(float x) {
  auto s = __builtin_amdgcn_permlane16_swap(__float_as_uint(x), __float_as_uint(x), false, false);
  x = vmax(__uint_as_float(s[0]), __uint_as_float(s[1]));
  auto t = __builtin_amdgcn_permlane32_swap(__float_as_uint(x), __float_as_uint(x), false, false);
  return vmax(__uint_as_float(t[0]), __uint_as_float(t[1]));
}
__device__ __forceinline__ float row_sum(float x) { x += dpp<XOR1>(x); x += dpp<XOR2>(x); x += dpp<ROR4>(x); x += dpp<ROR8>(x); return x; }
__device__ __forceinline__ unsigned now() { return (unsigned)__builtin_amdgcn_s_memrealtime(); }
__device__ __forceinline__ int wave_latest(unsigned t, unsigned ref) {
  int d = (int)(t - ref);
  d = max(d, (int)dppu<XOR1>((unsigned)d)); d = max(d, (int)dppu<XOR2>((unsigned)d)); d = max(d, (int)dppu<ROR4>((unsigned)d)); d = max(d, (int)dppu<ROR8>((unsigned)d));
  const int a = __builtin_amdgcn_readlane(d, 0), b = __builtin_amdgcn_readlane(d, 16), c = __builtin_amdgcn_readlane(d, 32), e = __builtin_amdgcn_readlane(d, 48);
  return min(max(max(a, b), max(c, e)), (int)(now() - ref));
}
__device__ __forceinline__ void wait_until(unsigned t) { while ((int)(now() - t) < 0) {} }
__device__ __forceinline__ void doze_until(unsigned t) { while ((int)(now() - t) < 0) __builtin_amdgcn_s_sleep(1); }
__device__ __forceinline__ unsigned lds_time(LAS unsigned char* p) {
  for (unsigned n = 0; n < LDS_CAP; ++n) {
    asm volatile("" ::: "memory");
    const v2u x = *(volatile LAS v2u*)p;
    if (__builtin_amdgcn_readfirstlane(x.y)) return __builtin_amdgcn_readfirstlane(x.x);
  }
  return now();
}
__device__ __forceinline__ void lds_count(LAS unsigned char* p, unsigned want) {
  for (unsigned n = 0; n < LDS_CAP; ++n) {
    asm volatile("" ::: "memory");
    if (__builtin_amdgcn_readfirstlane(*(volatile LAS unsigned*)p) >= want) break;
  }
  asm volatile("" ::: "memory");
}
__device__ __forceinline__ unsigned sane(unsigned t) { const unsigned n = now(); return (int)(t - n) > 0 ? n : t; }

typedef unsigned v16u __attribute__((ext_vector_type(16)));
__device__ __forceinline__ unsigned poll16(const unsigned char* p, unsigned tag, unsigned until) {
  for (;;) {
    v16u a;
    asm volatile("s_load_dwordx16 %0, %1, 0x0 glc\n\ts_waitcnt lgkmcnt(0)" : "=&s"(a) : "s"(p) : "memory");
    unsigned bad = 0;
#pragma unroll
    for (int i = 0; i < 16; ++i) bad |= a[i] ^ (tag << 16);
    const unsigned n = now();
    if (bad < 0x10000u) {
      unsigned d = 0xFFFFu;
#pragma unroll
      for (int i = 0; i < 16; ++i) d = min(d, (n - a[i]) & 0xFFFFu);
      return n - d;
    }
    if ((int)(n - until) >= 0) return n;
    __builtin_amdgcn_s_sleep(GS);
  }
}
__device__ __forceinline__ unsigned poll4(const unsigned char* p, unsigned tag, unsigned& spins, bool& dead) {
  for (;;) {
    v8u a;
    asm volatile("s_load_dwordx8 %0, %1, 0x0 glc\n\ts_waitcnt lgkmcnt(0)" : "=&s"(a) : "s"(p) : "memory");
    if ((((a[1] ^ tag) | (a[3] ^ tag)) | ((a[5] ^ tag) | (a[7] ^ tag))) == 0u || dead)
      return sane(a[0] + (unsigned)max(max(0, (int)(a[2] - a[0])), max((int)(a[4] - a[0]), (int)(a[6] - a[0]))));
    if (++spins > SPIN_CAP) dead = true;
    __builtin_amdgcn_s_sleep(POLL_SLEEP);
  }
}
__device__ __forceinline__ unsigned ld1(rsrc_t r, int vo, int so) { return __builtin_amdgcn_raw_buffer_load_b32(r, vo, so, AUX_SC1); }
__device__ __forceinline__ v2u ld2(rsrc_t r, int vo, int so) { return __builtin_amdgcn_raw_buffer_load_b64(r, vo, so, AUX_SC1); }
__device__ __forceinline__ v3u ld3(rsrc_t r, int vo, int so) { return __builtin_amdgcn_raw_buffer_load_b96(r, vo, so, AUX_SC1); }
__device__ __forceinline__ v4u ld4(rsrc_t r, int vo, int so) { return __builtin_amdgcn_raw_buffer_load_b128(r, vo, so, AUX_SC1); }
__device__ __forceinline__ void st1(unsigned v, rsrc_t r, int vo, int so) { __builtin_amdgcn_raw_buffer_store_b32(v, r, vo, so, AUX_SC1); }
__device__ __forceinline__ void st2(v2u v, rsrc_t r, int vo, int so) { __builtin_amdgcn_raw_buffer_store_b64(v, r, vo, so, AUX_SC1); }
__device__ __forceinline__ void st4(v4u v, rsrc_t r, int vo, int so) { __builtin_amdgcn_raw_buffer_store_b128(v, r, vo, so, AUX_SC1); }
__device__ __forceinline__ void st4c(rsrc_t r, int vo, int so) { st4(v4u{SENT, SENT, SENT, SENT}, r, vo, so); }

#define BARRIER() asm volatile("s_waitcnt lgkmcnt(0)\n\ts_barrier" ::: "memory")
#define BARRIER_VM() asm volatile("s_waitcnt vmcnt(0) lgkmcnt(0)\n\ts_barrier" ::: "memory")
#define FENCE() asm volatile("" ::: "memory")
#define SCHED() __builtin_amdgcn_sched_barrier(0)

__device__ __forceinline__ void dma(v4u rs, int voff, int soff, unsigned lds) {
  asm volatile("s_mov_b32 m0, %3\n\ts_nop 0\n\tbuffer_load_dwordx4 %0, %1, %2 offen nt lds" :: "v"(voff), "s"(rs), "s"(soff), "s"(lds) : "memory");
}
__device__ __forceinline__ v4u mkrs(const void* p, unsigned bytes) {
  const unsigned long long a = (unsigned long long)p;
  return v4u{(unsigned)a, (unsigned)(a >> 32) & 0xFFFFu, bytes, 0x00020000u};
}
__device__ __forceinline__ constexpr int prow(int R) { return 32 * (R >> 2) + 16 * (R & 1) + 4 * ((R >> 1) & 1); }
__device__ __forceinline__ void clear_rec(rsrc_t ws, int rec, int nbytes, int t) {
  if (t * 16 < nbytes) st4c(ws, t * 16, rec);
}
__device__ __forceinline__ v4u wfrag(LAS unsigned char* L, int off, int pst) {
  const s4v a = __builtin_amdgcn_ds_read_tr16_b64_v4i16((LAS s4v*)(L + off));
  const s4v b = __builtin_amdgcn_ds_read_tr16_b64_v4i16((LAS s4v*)(L + off + 2 * pst));
  const v2u x = __builtin_bit_cast(v2u, a), y = __builtin_bit_cast(v2u, b);
  return v4u{x.x, x.y, y.x, y.y};
}
__device__ __forceinline__ v8u cat(v4u a, v4u b) { return __builtin_shufflevector(a, b, 0, 1, 2, 3, 4, 5, 6, 7); }
__device__ __forceinline__ float gelu(float v) {
  const float u = v * (1.0f + 0.044715f * v * v) * (-2.0f * 0.7978845608028654f * 1.4426950408889634f);
  return v * __builtin_amdgcn_rcpf(1.0f + __builtin_amdgcn_exp2f(u));
}
}

#define STAMP(k) do {} while (0)
#define STAMPH(k) do {} while (0)
#define STAMPA(k) do {} while (0)
#define STAMPV(k, v) do {} while (0)
#define RETRY() { if (++spins > SPIN_CAP) dead = true; __builtin_amdgcn_s_sleep(BACKOFF); }
#define MISS(k) do {} while (0)
#define RESYNC(nx) do { const int d_ = (int)(now() + RSTOL - (O + (nx))); if (d_ > 0) O += d_ + RSX; } while (0)

__global__ void __launch_bounds__(512, 2)
blk_kernel(const u16* __restrict__ x0, const u16* __restrict__ kc, const u16* __restrict__ vc, const u16* __restrict__ wq,
           const u16* __restrict__ wo, const u16* __restrict__ w1, const u16* __restrict__ b1, const u16* __restrict__ w2,
           const u16* __restrict__ b2, u16* __restrict__ out, unsigned char* wsp) {
  __shared__ __attribute__((aligned(16))) unsigned char smem[L_END + 128 * STAMPS];
  const int tid = threadIdx.x, lane = tid & 63, w = __builtin_amdgcn_readfirstlane(tid >> 6);
  int c = blockIdx.x;
  const size_t kvoff = ((size_t)(((c >> 5) & 3) * 16 + (c & 7) + 8 * (c >> 7)) * SK + (SK - WIN) + ((c >> 3) & 3) * 512) * 128;
  const rsrc_t rk = __builtin_amdgcn_make_buffer_rsrc((void*)(kc + kvoff), 0, 131072, 0x00020000);
  const int ubase = (w >> 2) * 64 + (w & 3) * 4 + ROT * c;
  v4u kr[16], vr[16];
#define UNIT(jj) (((ubase + ((jj) >> 2) * 16 + ((jj) & 3)) & 127) * 1024)
#define K_LOADS(q0, q1) do { \
    _Pragma("unroll") for (int q = (q0); q < (q1); ++q) kr[q] = __builtin_amdgcn_raw_buffer_load_b128(rk, lane * 16, UNIT(q), AUX_NT); \
    SCHED(); } while (0)
#define V_LOADS(q0, q1) do { \
    _Pragma("unroll") for (int q = (q0); q < (q1); ++q) vr[q] = __builtin_amdgcn_raw_buffer_load_b128(rv, lane * 16, UNIT(q), AUX_NT); \
    SCHED(); } while (0)
  K_LOADS(0, PL);
  asm volatile("" : "+s"(c));
  unsigned long long pc0; asm volatile("s_getpc_b64 %0" : "=s"(pc0));
  pc0 &= ~255ull;
  LAS unsigned char* L = (LAS unsigned char*)smem;
  const unsigned lbase = (unsigned)(size_t)L;
  const int col = lane & 15, quad = lane >> 4;
  const int xcd = c & 7, j = c >> 3, g = xcd + 8 * (j >> 4), m = j & 15, ab = m >> 2, as = m & 3;
  const bool oc = m < 9, mlp = j >= 5;
  const int mu = mlp ? j - 5 : 0, mt = mu / 9, mi = mu - 9 * mt, mf = xcd * 3 + mt;
  const rsrc_t ws = __builtin_amdgcn_make_buffer_rsrc(wsp, 0, WS_END, 0x00020000);
  const rsrc_t rv = __builtin_amdgcn_make_buffer_rsrc((void*)(vc + kvoff), 0, 131072, 0x00020000);
#define K_RELAYOUT(u) do { LAS unsigned char* S = L + L_STG + w * 8192; \
    _Pragma("unroll") for (int jj = 0; jj < 8; ++jj) { const int i = 8 * (quad & 1) + jj; *(LAS v4u*)(S + (quad >> 1) * 4096 + i * 256 + ((col ^ i) * 16)) = kr[8 * (u) + jj]; } \
    _Pragma("unroll") for (int t = 0; t < 4; ++t) { \
      const v4u lo = *(LAS v4u*)(S + (t >> 1) * 4096 + col * 256 + (((8 * (t & 1) + quad) ^ col) * 16)); \
      const v4u hi = *(LAS v4u*)(S + (t >> 1) * 4096 + col * 256 + (((8 * (t & 1) + 4 + quad) ^ col) * 16)); \
      kb[4 * (u) + t] = __builtin_shufflevector(lo, hi, 0, 1, 2, 3, 4, 5, 6, 7); } \
    SCHED(); } while (0)
#define SPOS(b, k) (((k) >> 6) * SPS + (2 * (b) + (((k) >> 1) & 1)) * SPR + (((k) >> 4) & 3) * 16 + (((k) >> 2) & 3) * 4)
#define QPOS(h, d) ((((d) >> 6) & 1) * 256 + ((((d) >> 1) & 1) * 2 + (h)) * 64 + (((d) >> 4) & 3) * 16 + (((d) >> 2) & 3) * 4)
  const int g1 = j >> 4, pslot = mi < 8 ? (mi >> 2) * BLK + (mi & 3) * 1536 : 4 * 1536;
  const int rC0 = WS_CD + m * 3 * BLK + g * 512, rD0 = WS_CD + (mi * 3 + 1 + (mf >> 4)) * BLK + (mf & 15) * 512;
  unsigned sides = 0;
  int PB = 0, sd = 0;
#define SET_SIDES(x) do { sides = (x); sides |= (~sides & 15u) << 4; \
    sd = ((sides >> xcd) & 1u) * 8192; PB = WS_S + (xcd & 3) * 17 * BLK + sd; } while (0)
#define BSLOT(r) (((xcd >> 2) * 18 + g1 * 9 + (r)) * 128)
#define RA2 (PB + (g1 * 4 + (m >> 2)) * BLK + (m & 3) * 2048)
#define RST (PB + 16 * BLK + g1 * 2048)
#define RB(Lr) ((Lr) ? PB + (8 + g1) * BLK : WS_B1 + g * 8192)
#define RL(Lr) ((Lr) ? RST + 4096 : WS_L1 + g * 2048)
#define RP(Lr) ((Lr) ? PB + (10 + 2 * mt) * BLK : WS_P1 + mf * 32768)
  const int dr = 8 * (lane >> 5) + ((lane >> 3) & 3), dc = ((((lane & 7) >> 1) ^ (lane >> 4)) * 2 + (lane & 1)) * 16;
#define DMA_WO(wv) do { const v4u ro_ = mkrs(wo + (size_t)(256 * g) * D + 64 * m, 256 * D * 2); \
    _Pragma("unroll") for (int p = 0; p < 8; ++p) dma(ro_, dr * (D * 2) + dc, (64 * (wv) + prow(p)) * (D * 2), lbase + L_WO + ((wv) * 8 + p) * 1024); } while (0)
#define DMA_W1(wv) do { const v4u r1_ = mkrs(w1 + (size_t)(64 * mi) * F + 192 * mf, 64 * F * 2); \
    _Pragma("unroll") for (int p = 0; p < 8; ++p) { const int q_ = (wv) * 8 + p, R_ = (q_ * 43) >> 7; dma(r1_, dr * (F * 2) + dc, prow(R_) * (F * 2) + (q_ - 3 * R_) * 128, lbase + L_W1 + q_ * 1024); } } while (0)
#define DMA_W2(wv, p0, p1) do { const v4u r2_ = mkrs(w2 + (size_t)(192 * mf) * D + 64 * mi, 192 * D * 2); \
    _Pragma("unroll") for (int p = (p0); p < (p1); ++p) dma(r2_, dr * (D * 2) + dc, (64 * (wv) + prow(p)) * (D * 2), lbase + L_W2 + ((wv) * 8 + p) * 1024); } while (0)
  const unsigned t0 = now();
  const f4 Z4 = {0.f, 0.f, 0.f, 0.f};
  v8u kb[8], vb[8];
  unsigned tag;
  int sidx, lq[4], lO, lML;
#define ATT_CONSTS() do { sidx = (col & 2) ? 0xEEEE : 0x4444; \
    _Pragma("unroll") for (int T = 0; T < 4; ++T) lq[T] = L_Q + ((col >> 2) == T ? (col & 3) * 64 + quad * 16 : 512); \
    lO = L_O + w * 1024 + lane * 8; lML = lane < 2 ? L_ML + lane * 64 + w * 8 : L_DUM; \
    asm volatile("" : "+v"(sidx), "+v"(lq[0]), "+v"(lq[1]), "+v"(lq[2]), "+v"(lq[3]), "+v"(lO), "+v"(lML)); } while (0)
#define PT123() do { pt1 = v4u{dppb<0x124, 2>(pt1.x, X), dppb<0xE4, 2>(pt1.y, X), dppb<0x12C, 2>(pt1.z, X), dppb<0x128, 2>(pt1.w, X)}; \
    pt2 = v4u{dppb<0x128, 4>(pt2.x, X), dppb<0x124, 4>(pt2.y, X), dppb<0xE4, 4>(pt2.z, X), dppb<0x12C, 4>(pt2.w, X)}; \
    pt3 = v4u{dppb<0x12C, 8>(pt3.x, X), dppb<0x128, 8>(pt3.y, X), dppb<0x124, 8>(pt3.z, X), dppb<0xE4, 8>(pt3.w, X)}; } while (0)
#define ATT_PREP(PIN) \
    v4u pt0 = {0u, 0u, 0u, 0u}, pt1 = pt0, pt2 = pt0, pt3 = pt0; f4 sc = Z4, ls = Z4, e0 = Z4, e1 = Z4; \
    v8u one8 = {ONE2, ONE2, ONE2, ONE2, ONE2, ONE2, ONE2, ONE2}; \
    if (PIN) asm volatile("" : "+v"(pt0), "+v"(pt1), "+v"(pt2), "+v"(pt3), "+v"(sc), "+v"(ls), "+v"(e0), "+v"(e1), "+v"(one8))
#define ATT_QK() \
    { v4u qa[8];                                              \
      _Pragma("unroll") for (int T = 0; T < 4; ++T) { qa[2 * T] = *(LAS v4u*)(L + lq[T]); qa[2 * T + 1] = *(LAS v4u*)(L + lq[T] + 256); } \
      SCHED(); \
      _Pragma("unroll") for (int t = 0; t < 8; ++t) sc = smfma(qa[t], kb[t], sc, sidx); } \
    const float s0 = sc.x + sc.z, s1 = sc.y + sc.w;           \
    const bool odd = col & 1;                                 \
    const float sa = odd ? s1 : s0, sb = dpp<XOR1>(odd ? s0 : s1); \
    const float mx = par_max(vmax(sa, sb)) * SCL; \
    const unsigned X = cvtpk(__builtin_amdgcn_exp2f(fmaf(odd ? sb : sa, SCL, -mx)), __builtin_amdgcn_exp2f(fmaf(odd ? sa : sb, SCL, -mx))); \
      \
    pt0 = v4u{dppb<0xE4, 1>(pt0.x, X), dppb<0x12C, 1>(pt0.y, X), dppb<0x128, 1>(pt0.z, X), dppb<0x124, 1>(pt0.w, X)}
#define ATT_L() do { ls = smfma(pt0, one8, ls, sidx); *(LAS f2*)(L + lML) = f2{mx, odd ? ls.y + ls.w : ls.x + ls.z}; } while (0)
#define V_TRANSPOSE() \
    _Pragma("unroll") for (int tt = 0; tt < 8; ++tt) { \
      SCHED(); \
      asm volatile("" : "+v"(vr[2 * tt]), "+v"(vr[2 * tt + 1])); \
      if (tt == 7) { STAMPH(26); } \
      _Pragma("unroll") for (int mm = 0; mm < 4; ++mm) { \
        vb[2 * mm][tt] = __builtin_amdgcn_perm(vr[2 * tt + 1][mm], vr[2 * tt][mm], 0x05040100u); \
        vb[2 * mm + 1][tt] = __builtin_amdgcn_perm(vr[2 * tt + 1][mm], vr[2 * tt][mm], 0x07060302u); \
        asm volatile("" :: "v"(vb[2 * mm][tt]), "v"(vb[2 * mm + 1][tt])); } } \
    SCHED()
#define ATT_PV(MID) do { \
    e0 = smfma(pt0, vb[0], e0, sidx); e1 = smfma(pt0, vb[1], e1, sidx); \
    MID; \
    e0 = smfma(pt1, vb[2], e0, sidx); e1 = smfma(pt1, vb[3], e1, sidx); \
    e0 = smfma(pt2, vb[4], e0, sidx); e1 = smfma(pt2, vb[5], e1, sidx); \
    e0 = smfma(pt3, vb[6], e0, sidx); e1 = smfma(pt3, vb[7], e1, sidx); \
    *(LAS f2*)(L + lO) = f2{sadd(e0.x, e0.z), sadd(e1.x, e1.z)}; \
    *(LAS f2*)(L + lO + 512) = f2{sadd(e0.y, e0.w), sadd(e1.y, e1.w)}; } while (0)
#define WAIT_Q() lds_count(L + L_QF, 2u)
#define LDS_ADD(off) (void)__hip_atomic_fetch_add((LAS unsigned*)(L + (lane == 0 ? (off) : L_DUM + lane * 4)), 1u, __ATOMIC_RELAXED, __HIP_MEMORY_SCOPE_WORKGROUP)
#define V_ISSUED() LDS_ADD(L_CNT + 8)
  if (__builtin_expect(w >= 4, 0)) {
    const int wv = w - 4;
#define CLEAR_L2(wv) do { int o1_, o2_; const int h_ = (lane >> 5) * 8192 + (lane & 31) * 16; \
      if (wv < 2) { o1_ = RA2 + wv * 1024 + lane * 16; \
        o2_ = wv == 0 ? (lane < 32 ? RB(1) + m * 512 + lane * 16 : lane < 40 ? RL(1) + m * 128 + (lane - 32) * 16 : lane < 48 ? RST + m * 128 + (lane - 40) * 16 : OOB) \
                      : (oc ? rC0 + 27 * BLK + h_ : OOB); } \
      else if (wv == 2) { o1_ = mlp ? RP(1) + pslot + lane * 16 : OOB; o2_ = mlp ? rD0 + 27 * BLK + h_ : OOB; } \
      else { o1_ = mlp ? (lane < 32 ? RP(1) + pslot + 1024 + lane * 16 : lane < 40 ? RP(1) + BLK + 6144 + mi * 128 + (lane - 32) * 16 : OOB) : OOB; \
        o2_ = mlp && mf == 0 ? WS_CD + (27 + mi * 3 + 2) * BLK + 4096 + h_ : OOB; \
        if (oc && g == 0) st4c(ws, h_, WS_CD + (27 + m * 3 + 2) * BLK + 4608); } \
      st4c(ws, o1_, 0); st4c(ws, o2_, 0); } while (0)
    V_LOADS(0, 1); K_LOADS(PL, 3); V_LOADS(1, HV);
    asm volatile("s_mov_b32 m0, %2\n\ts_nop 0\n\tglobal_load_lds_dwordx4 %0, %1" :: "v"(lane * 16 + ((j * 4 + wv) % CODE_KB) * 1024), "s"(pc0), "s"(lbase + L_O + wv * 1024) : "memory");
    tag = ld1(ws, 0, 0);
    if (w == 5) {
      const unsigned char* tp_ = lane == 0 ? (const unsigned char*)(wo + (size_t)(256 * g) * D) : lane == 1 ? (const unsigned char*)(wo + (size_t)(256 * g + 255) * D)
          : lane == 2 ? (const unsigned char*)(w1 + (size_t)(64 * mi) * F) : lane == 3 ? (const unsigned char*)(w1 + (size_t)(64 * mi + 63) * F)
          : lane == 4 ? (const unsigned char*)(w2 + (size_t)(192 * mf) * D) : lane == 5 ? (const unsigned char*)(w2 + (size_t)(192 * mf + 191) * D)
          : lane == 6 ? (const unsigned char*)out : lane == 7 ? (const unsigned char*)b1 : lane == 8 ? (const unsigned char*)b2 : wsp + WS_TCH;
      asm volatile("s_mov_b32 m0, %1\n\ts_nop 0\n\tglobal_load_lds_dword %0, off" :: "v"(tp_), "s"(lbase + L_UO) : "memory");
    }
    if (w == 4 && lane < 24) *(LAS unsigned*)(L + L_CNT + lane * 4) = 0u;
    BARRIER();
#define CALIBRATE(CADD) do { \
    if (NCAL && w == 7 && c < 4) {                           \
      const unsigned char* p = wsp + WS_CAL + c * 128; \
      unsigned la = ~0u, lb = ~0u; \
_Pragma("unroll 1") \
      for (int r = 0; r < NCAL; ++r) { \
        unsigned long long t1, t2, t3; unsigned v; \
        asm volatile("s_memtime %0\n\ts_waitcnt lgkmcnt(0)\n\ts_load_dword %3, %4, 0x0 glc\n\ts_waitcnt lgkmcnt(0)\n\ts_memtime %1\n\ts_waitcnt lgkmcnt(0)\n\t" \
                     "s_load_dword %3, %4, 0x2000 glc\n\ts_waitcnt lgkmcnt(0)\n\ts_memtime %2\n\ts_waitcnt lgkmcnt(0)" : "=&s"(t1), "=&s"(t2), "=&s"(t3), "=&s"(v) : "s"(p) : "memory"); \
        la = min(la, (unsigned)(t2 - t1)); lb = min(lb, (unsigned)(t3 - t2)); \
      } \
      st2(v2u{(lb < la) != (FLIP != 0) ? 1u : 0u, tag + CADD}, ws, lane == 0 ? WS_SIDE + c * 128 : OOB, 0); \
    } \
    SCHED(); } while (0)
    if (CPOS == 0) CALIBRATE(1u);
    V_LOADS(HV, 3); K_LOADS(3, 8);
    if (CPOS == 1) CALIBRATE(1u);
    V_LOADS(3, 4); K_LOADS(8, 12); V_LOADS(4, 6); K_LOADS(12, 16);
    if (CPOS == 2) CALIBRATE(1u);
    K_RELAYOUT(0); ATT_CONSTS(); tag += 1u;
    V_LOADS(6, 16 - HL);
    K_RELAYOUT(1);
    lds_count(L + L_CNT + 8, 4u);
    V_LOADS(16 - HL, 16);
    v2u sw = ld2(ws, w == 4 ? WS_SIDE + (lane & 3) * 128 : OOB, 0);
    SCHED();
    {
      V_ISSUED();
      STAMPH(28);
      ATT_PREP(false);
      WAIT_Q();
      ATT_QK();
      ATT_L();
      PT123();
      SCHED();
      asm volatile("" : "+v"(pt0), "+v"(pt1), "+v"(pt2), "+v"(pt3), "+v"(e0), "+v"(e1));
      V_TRANSPOSE();
      ATT_PV();
      if (w == 4) {
        unsigned sb = (unsigned)__ballot(sw.x & 1u) & 15u;
        if (!__all(sw.y == tag))
          for (unsigned n = 0; n < SPIN_CAP; ++n) {
            __builtin_amdgcn_s_sleep(8); FENCE();
            const v2u x = ld2(ws, WS_SIDE + (lane & 3) * 128, 0);
            sb = (unsigned)__ballot(x.x & 1u) & 15u;
            if (__all(x.y == tag)) break;
          }
        *(LAS unsigned*)(L + L_QF + 8) = sb;
      }
    }
    BARRIER();
    SET_SIDES(__builtin_amdgcn_readfirstlane(*(volatile LAS unsigned*)(L + L_QF + 8)));
    if (w == 4) st2(v2u{now(), tag}, ws, lane < 9 ? WS_BCN + (xcd & 3) * BLK + sd + BSLOT(lane) + m * 8 : OOB, 0);
    if (w == 5) {
      const unsigned tv = now();
      const unsigned garr = __builtin_amdgcn_raw_ptr_buffer_atomic_add_i32(1, ws, lane == 0 ? WS_CNT + g * 128 : OOB, 0, AUX_SC1 | 1);
      if (__builtin_amdgcn_readfirstlane(garr) == 14u) {
        st1((tag << 16) | (tv & 0xFFFFu), ws, WS_BCN + (lane & 3) * BLK + ((sides >> (lane & 7)) & 1u) * 8192 + 5120 + ((lane & 4) * 2 + (lane >> 3)) * 128 + g * 4, 0);
        STAMPA(27);
      }
    }
    if (!mlp) CLEAR_L2(wv);
    if (oc && !__builtin_amdgcn_readfirstlane(*(LAS unsigned*)(L + L_CNT + 32 + 4 * wv))) { doze_until(t0 + G1); DMA_WO(wv); }
    asm volatile("s_waitcnt vmcnt(0)" ::: "memory");
    if (oc) {
      LDS_ADD(L_CNT + 12);
      if (mlp && wv < 3) { lds_count(L + L_GO, 4u); doze_until(now() + W2GAP); }
    } else if (wv < 3) doze_until(t0 + G1);
    if (mlp && wv < 3) {
      DMA_W1(wv);
      DMA_W2(wv, 0, W2A);
      asm volatile("s_waitcnt vmcnt(%0)" :: "n"(W2A) : "memory");
      const v2u an = *(volatile LAS v2u*)(L + L_CNT + 24);
      if (__builtin_amdgcn_readfirstlane(an.y)) {
        const unsigned tc = __builtin_amdgcn_readfirstlane(an.x) + SW_C1;
        if ((int)(now() + W2LEAD - tc) > 0) doze_until(tc + W2GAP);
      }
      DMA_W2(wv, W2A, 8);
    }
    if (w == 7) {
      const unsigned r = poll16(wsp + WS_BCN + (xcd & 3) * BLK + sd + 5120 + ((xcd >> 2) * 8 + (j & 7)) * 128, __builtin_amdgcn_readfirstlane(tag), t0 + 20000);
      *(LAS v2u*)(L + L_CNT + 24) = v2u{FIXO ? t0 + FIXO : r, 1u};
    }
#define STATS(r0, st, rec) do { if (w == 4) { \
      const v4u u0_ = *(LAS v4u*)(L + (r0)), u1_ = *(LAS v4u*)(L + (r0) + (st)); \
      f4 s_ = mfma(u0_, u0_, Z4); s_ = mfma(u1_, u1_, s_); \
      st1(nosent(__float_as_uint(quad == sq0 ? s_.x : quad == sq1 ? s_.y : quad == sq2 ? s_.z : s_.w)), ws, sS, (rec)); } } while (0)
    int sS = col < 4 && quad < 2 ? 8 * col + (quad == 0 ? 4 : 0) : OOB, lS1 = L_U1 + 2 * (col < 4 ? col : 4) * SPR + quad * 16, lSX = L_UX + (col < 4 ? col : 4) * UXS + quad * 16;
    int sq0 = col == 0 ? 0 : 1, sq1 = col == 1 ? 0 : 5, sq2 = col == 2 ? 0 : 5;
    if (col == 0) sq0 = quad;
    asm volatile("" : "+v"(sS), "+v"(lS1), "+v"(lSX), "+v"(sq0), "+v"(sq1), "+v"(sq2));
    if (mlp) { if (w == 7) { CLEAR_L2(0); CLEAR_L2(1); CLEAR_L2(2); CLEAR_L2(3); asm volatile("s_waitcnt vmcnt(0)" ::: "memory"); } BARRIER(); STATS(lS1, SPR, RP(0) + BLK + 6144 + mi * 128); }
    if (mlp) BARRIER_VM();
    if (wv < 2) {
      const unsigned smk = (col & 2) ? 0u : (col & 1) ? 0x3F800000u : 0x3F80u;
      const v4u sl = {(col >> 2) == 0 ? smk : 0u, (col >> 2) == 1 ? smk : 0u, (col >> 2) == 2 ? smk : 0u, (col >> 2) == 3 ? smk : 0u};
      if (!mlp) doze_until(lds_time(L + L_CNT + 24) + SW_P1 + 60);
      for (unsigned sp = 0; sp < SPIN_CAP; ++sp) {
        FENCE();
        v4u z[4];
#pragma unroll
        for (int i = 0; i < 4; ++i) z[i] = ld4(ws, quad * A1S + 2048 + wv * 256 + col * 16, WS_A1 + (g * 16 + 4 * i) * A1S);
        f4 a = mfma(sl, z[0], Z4); a = mfma(sl, z[1], a); a = mfma(sl, z[2], a); a = mfma(sl, z[3], a);
        *(LAS f2*)(L + L_CS + wv * 512 + lane * 8) = f2{a.x, a.y};
        if (__all(umax4(umax4(umax4(umax4(0u, z[0]), z[1]), z[2]), z[3]) != SENT)) break;
        __builtin_amdgcn_s_sleep(8);
      }
    }
    BARRIER();
    STATS(lSX, 64, RST + m * 128);
    {
      ATT_PREP(true);
      BARRIER();
      ATT_QK(); PT123(); ATT_PV(ATT_L());
    }
    BARRIER();
    if (oc) BARRIER();
    if (mlp) { BARRIER(); STATS(lS1, SPR, RP(1) + BLK + 6144 + mi * 128); BARRIER(); }
    return;
  }

  __builtin_amdgcn_s_setprio(3);
  STAMP(0);
  bool dead = false;
  unsigned spins = 0, miss = 0, tout = 0, Oanch = 0; (void)miss; (void)tout; (void)Oanch;
  const int trr = 4 * (quad & 1) + (col >> 2), hq = quad >> 1;
#define TRO(s) (trr * 128 + ((((s) ^ (trr >> 1)) & 3) * 32) + (lane & 3) * 8)
#define TRQ(hp, e) (trr * 128 + ((((2 * (hp) + ((lane & 3) >> 1)) ^ (trr >> 1)) * 2 + (lane & 1)) * 16) + (e) * 8)
#define WQ0(t) wfrag(L, TRQ((t) >> 1, (t) & 1) + hq * 4096 + L_WQ + w * 1024, 4096)
#define WQ1(t) wfrag(L, TRQ((t) >> 1, (t) & 1) + 16384 + L_WQ + w * 1024, 0)
  v2u csr;
  {
    const int tp = col >> 2, xb_ = col & 3, rec = WS_A1 + (g * 16 + m) * A1S;
    const int so = xb_ * 512 + (64 * w + 32 * (tp >> 1) + 8 * quad + 4 * (tp & 1)) * 2;
    if (w == 3 && c < 4) st1(0u, ws, lane < 2 ? WS_CAL + c * 128 + lane * 8192 : OOB, 0);
    tag = ld1(ws, 0, 0);
    const v4u rq = mkrs(wq + (size_t)(36 * m) * NQ + 256 * g + 64 * w, 36 * NQ * 2);
#pragma unroll
    for (int p = 0; p < 5; ++p) dma(rq, dr * (NQ * 2) + dc, prow(p) * (NQ * 2), lbase + L_WQ + (p * 4 + w) * 1024);
    const rsrc_t rx = __builtin_amdgcn_make_buffer_rsrc((void*)x0, 0, 4 * D * 2, 0x00020000);
    const v4u xa = __builtin_amdgcn_raw_buffer_load_b128(rx, lane * 16, w * (D * 2), 0);
    const v4u xc = __builtin_amdgcn_raw_buffer_load_b128(rx, lane < 8 ? 1024 + lane * 16 : OOB, w * (D * 2), 0);
    v4u u0 = __builtin_amdgcn_raw_buffer_load_b128(rx, xb_ * (D * 2) + quad * 16, 72 * m, 0);
    const v2u ut = __builtin_amdgcn_raw_buffer_load_b64(rx, xb_ * (D * 2) + 64, 72 * m, 0);
    SCHED();
    st4c(ws, lane < 40 ? rec + (lane >> 3) * 512 + w * 128 + (lane & 7) * 16 : w != 3 ? OOB : lane < 48 ? rec + 2560 + (lane & 7) * 16 : lane < 56 && c < 4 ? WS_SIDE + c * 128 + (lane & 7) * 16 : OOB, 0);
    {
      int o;
      const int h = (lane >> 5) * 8192 + (lane & 31) * 16;
      int o2;
      if (w == 0) { o = lane < 32 ? RB(0) + m * 512 + lane * 16 : lane < 40 ? RL(0) + m * 128 + (lane - 32) * 16 : lane < 48 && m == 0 ? WS_CNT + g * 128 + (lane - 40) * 16 : OOB;
                    o2 = mlp && mf == 0 ? WS_CD + (mi * 3 + 2) * BLK + 4096 + h : OOB; }
      else if (w == 1) { o = oc ? rC0 + h : OOB; o2 = mlp ? rD0 + h : OOB; }
      else if (w == 2) { o = mlp ? RP(0) + pslot + lane * 16 : OOB;
                         o2 = WS_BCN + (xcd & 3) * BLK + ((lane >> 3) & 1) * 8192 + (lane & 7) * 16 + (lane < 16 ? (oc ? BSLOT(m) : OOB) : lane < 32 && j < 8 ? 5120 + ((xcd >> 2) * 8 + j) * 128 : OOB); }
      else { o = !mlp ? OOB : lane < 32 ? RP(0) + pslot + 1024 + lane * 16 : lane < 56 ? RP(0) + 8192 + mi * 384 + (lane - 32) * 16 : RP(0) + BLK + 6144 + mi * 128 + (lane - 56) * 16; o2 = OOB; }
      if (!(XP & 1) || w < 2) st4c(ws, o, 0);
      if ((w < 2 && !(XP & 2)) || (w == 2 && !(XP & 4))) st4c(ws, (XP & 8) && w == 2 && lane >= 16 ? OOB : o2, 0);
    }
    SCHED();
    asm volatile("s_barrier" ::: "memory");
    {
      float S = 0.f, Q = 0.f;
#pragma unroll
      for (int e = 0; e < 4; ++e) { S = dot2(xa[e], ONE2, S); Q = dot2(xa[e], xa[e], Q); S = dot2(xc[e], ONE2, S); Q = dot2(xc[e], xc[e], Q); }
      const float mn = xq_sum(row_sum(S)) * (1.0f / D), rs = __builtin_amdgcn_rsqf(fmaxf(xq_sum(row_sum(Q)) * (1.0f / D) - mn * mn, 0.f) + EPS);
      *(volatile LAS v2u*)(L + L_ML + w * 8) = v2u{__float_as_uint(mn), __float_as_uint(rs)};
      asm volatile("s_waitcnt vmcnt(0) lgkmcnt(0)" : "+v"(tag) :: "memory");
      tag += 1u;
      LDS_ADD(L_QF + 4);
    }
    v4u wa[8];
#pragma unroll
    for (int t = 0; t < 4; ++t) { wa[2 * t] = WQ0(t); wa[2 * t + 1] = WQ1(t); }
    lds_count(L + L_QF + 4, 4u);
    const v2u sx = *(volatile LAS v2u*)(L + L_ML + xb_ * 8);
    if (w == 3) st2(v2u{tag, tag}, ws, lane == 0 ? rec + 2560 : OOB, 0);
    const float mean = __uint_as_float(sx.x), rstd = __uint_as_float(sx.y);
#define NORM2(x) cvtpk((bf2f((x) & 0xFFFFu) - mean) * rstd, (bfhi(x) - mean) * rstd)
    u0 = v4u{NORM2(u0.x), NORM2(u0.y), NORM2(u0.z), NORM2(u0.w)};
    const v4u u1 = {quad ? 0u : NORM2(ut.x), quad ? 0u : NORM2(ut.y), 0u, 0u};
    f4 r = Z4;
#pragma unroll
    for (int t = 0; t < 4; ++t) { f4 a = mfma(wa[2 * t], u0, Z4); a = mfma(wa[2 * t + 1], u1, a); r = tp == t ? a : r; }
    {
      const unsigned p0 = nosent(cvtpk(r.x, r.y)), p1 = nosent(cvtpk(r.z, r.w));
      st4(v4u{p0, p1, dppu<0x104>(p0), dppu<0x104>(p1)}, ws, (tp & 1) ? OOB : so, rec);
    }
    STAMP(1);
    SCHED();
    K_LOADS(PL, 4); V_LOADS(0, 4); K_LOADS(4, 8);
    if (w < 2) wait_until(t0 + T_F);
    const int a1 = WS_A1 + g * 16 * A1S, fo = w < 2 ? ((quad & 1) ? col * 16 + mi : g * 16 + col) * A1S + 2560 : OOB, ao = w < 2 ? quad * A1S + ab * 512 + w * 256 + col * 16 : OOB;
    v2u fl = ld2(ws, fo, WS_A1);
    SCHED();
    V_LOADS(4, 8); K_LOADS(8, 12);
    K_RELAYOUT(0);
    V_LOADS(8, 12);
    const bool okf = __all(w >= 2 || fl.x == tag);
    v4u za[4];
#define A1_LOADS(vo) _Pragma("unroll") for (int i = 0; i < 4; ++i) za[i] = ld4(ws, (vo), a1 + 4 * i * A1S)
    A1_LOADS(okf ? ao : OOB);
    SCHED();
    K_LOADS(12, 16); V_LOADS(12, 16);
    V_ISSUED();
    r = Z4;
#pragma unroll
    for (int t = 0; t < 4; ++t) { f4 a = mfma(WQ0(t), v4u{ONE2, ONE2, ONE2, ONE2}, Z4); a = mfma(WQ1(t), v4u{quad ? 0u : ONE2, quad ? 0u : ONE2, 0u, 0u}, a); r = tp == t ? a : r; }
    csr = v2u{nosent(cvtpk(r.x, r.y)), nosent(cvtpk(r.z, r.w))};
    if (lane < 32) *(LAS unsigned*)(L + L_UX + w * UXS + lane * 4) = 0u;
    if (w == 0) {
      if (lane < 32) { *(LAS unsigned*)(L + L_U1 + (8 + (lane >> 4)) * SPR + (lane & 15) * 4) = ONE2; *(LAS unsigned*)(L + L_UX + 4 * UXS + lane * 4) = ONE2; }
      if (lane < 8) { *(LAS unsigned*)(L + L_Q + 512 + (lane >> 2) * 256 + (lane & 3) * 4) = 0u; }
    }
    SCHED();
    STAMP(2);
    {
      const unsigned smk = (col & 2) ? 0u : (col & 1) ? 0x3F800000u : 0x3F80u;
      const v4u sl = {(col >> 2) == 0 ? smk : 0u, (col >> 2) == 1 ? smk : 0u, (col >> 2) == 2 ? smk : 0u, (col >> 2) == 3 ? smk : 0u};
      f4 z; unsigned mx;
#define A1_SUM() do { z = mfma(sl, za[0], Z4); z = mfma(sl, za[1], z); z = mfma(sl, za[2], z); z = mfma(sl, za[3], z); \
        mx = umax4(umax4(umax4(umax4(0u, za[0]), za[1]), za[2]), za[3]); asm volatile("" : "+v"(z), "+v"(mx)); } while (0)
      A1_SUM();
      if (!okf) {
        MISS(0);
        for (;;) { FENCE(); fl = ld2(ws, fo, WS_A1); if (__all(fl.x == tag) || dead) break; RETRY(); __builtin_amdgcn_s_sleep(16); }
        mx = SENT;
      }
      while (!__all(mx != SENT) && !dead) { FENCE(); A1_LOADS(ao); A1_SUM(); if (__all(mx != SENT)) break; RETRY(); MISS(1); __builtin_amdgcn_s_sleep(16); }
      if (w < 2) {
        *(volatile LAS unsigned*)(L + L_Q + QPOS(w, 8 * col + 2 * quad)) = cvtpk(z.x, z.y);
        asm volatile("s_waitcnt lgkmcnt(0)" ::: "memory");
        LDS_ADD(L_QF);
      }
    }
    STAMP(29);
    K_RELAYOUT(1);
    if (w == 1) {
      const u16* cp = lane < 24 ? b1 + 192 * mf + 8 * lane : lane < 32 ? b2 + 64 * mi + 8 * (lane - 24) : x0 + ((lane - 32) >> 3) * D + 64 * mi + 8 * (lane & 7);
      asm volatile("s_mov_b32 m0, %1\n\ts_nop 0\n\tglobal_load_lds_dwordx4 %0, off" :: "v"(cp), "s"(lbase + L_CST) : "memory");
    }
  }
  __builtin_amdgcn_s_setprio(1);
  ATT_CONSTS();
  v4u sel, sel3;
  int sB, lMr, lOr, sBl, vB, vBl, vC, vCr, vP, vPt, vPs;
#define LANE_IDS() int ln = lane; asm volatile("" : "+v"(ln)); \
  const int col = ln & 15, quad = ln >> 4, gc = ln & 7, grb = (ln >> 3) & 1, grec = 2 * quad + grb, trr = 4 * (quad & 1) + (col >> 2), hq = quad >> 1; \
  const int gb = 2 * (gc >> 2) + (quad & 1), gn = 4 * (gc & 3) + 2 * (quad >> 1), lane = ln;     \
  (void)col; (void)grec; (void)gb; (void)gn; (void)trr; (void)hq; (void)lane; (void)grb
#define PAIR_T(x) ({ auto s_ = __builtin_amdgcn_permlane16_swap((x), (x), false, false); __builtin_amdgcn_perm(s_[1], s_[0], (quad & 1) ? 0x07060302u : 0x05040100u); })
  unsigned O = 0;
  float csh[3] = {0.f, 0.f, 0.f};
  int vD, vDr, vA, vAs;

#pragma unroll
  for (int Lr = 0; Lr < 2; ++Lr) {
    if (Lr == 0) {
      WAIT_Q();
      STAMP(3);
      { ATT_PREP(false); ATT_QK(); ATT_L(); V_TRANSPOSE(); PT123(); ATT_PV(); }
      SCHED();
      {
        const int gc = lane & 7, grb = (lane >> 3) & 1, grec = 2 * quad + grb, sl_ = 32 * (w & 1) + (lane >> 1), sr = (gc & 3) + 4 * grb + 8 * (quad >> 1);
        sB = (lane & 1) ? OOB : (w >> 1) * 256 + (4 * (sl_ & 15) + (sl_ >> 4)) * 4;
        lMr = L_ML + (w >> 1) * 64 + (lane & 7) * 8; lOr = L_O + w * 256 + lane * 4; sBl = lane == 0 && !(w & 1) ? 2 * w : OOB;
        vB = quad * 512 + col * 16; vBl = quad * 128;
        vC = grec * 512 + w * 128 + gc * 16; vCr = lane < 8 ? vC : OOB;
        vP = (grec >> 2) * BLK + (grec & 3) * 1536 + 3 * w * 128 + gc * 16; vPt = lane < 8 ? vP : OOB; vPs = sr < 9 ? BLK + 6144 + sr * 128 + 8 * (2 * (gc >> 2) + (quad & 1)) : OOB;
        const unsigned smk = (col & 2) ? 0u : (col & 1) ? 0x3F800000u : 0x3F80u, sm3 = (col & 2) ? 0u : (col & 4) ? 0x3F800000u : 0x3F80u;
        const int d3 = 2 * (col >> 3) + (col & 1);
        sel = v4u{(col >> 2) == 0 ? smk : 0u, (col >> 2) == 1 ? smk : 0u, (col >> 2) == 2 ? smk : 0u, (col >> 2) == 3 ? smk : 0u};
        sel3 = v4u{d3 == 0 ? sm3 : 0u, d3 == 1 ? sm3 : 0u, d3 == 2 ? sm3 : 0u, d3 == 3 ? sm3 : 0u};
        asm volatile("" : "+v"(sel), "+v"(sel3), "+v"(sB), "+v"(lMr), "+v"(lOr), "+v"(sBl), "+v"(vB), "+v"(vBl), "+v"(vC), "+v"(vCr), "+v"(vP), "+v"(vPt), "+v"(vPs));
      }
      SCHED();
      const unsigned pre = oc && (int)(now() - t0) >= PRE_T;
      if (pre) { lds_count(L + L_CNT + 8, 8u); DMA_WO(w); }
      *(LAS unsigned*)(L + L_CNT + 32 + 4 * w) = pre;
    } else {
      ATT_PREP(false);
      if (w < 2) {
        const int a2 = PB + g1 * 4 * BLK, as2 = RST;
        v2u st; v4u v0, v1, v2, v3;
#define LD_A() do { st = ld2(ws, vAs, as2); v0 = ld4(ws, vA, a2); v1 = ld4(ws, vA, a2 + BLK); v2 = ld4(ws, vA, a2 + 2 * BLK); v3 = ld4(ws, vA, a2 + 3 * BLK); } while (0)
        wait_until(O + SW_A2);
        LD_A();
        SCHED();
        int lQw = L_Q + QPOS(w, 8 * col + 2 * quad);
        f2 cs = *(LAS f2*)(L + L_CS + w * 512 + lane * 8);
        asm volatile("" : "+v"(lQw), "+v"(cs), "+v"(pt0), "+v"(pt1), "+v"(pt2), "+v"(pt3), "+v"(sc), "+v"(ls), "+v"(e0), "+v"(e1), "+v"(one8));
        SCHED();
        for (;;) {
          f4 a = mfma(sel, v0, Z4); a = mfma(sel, v1, a); a = mfma(sel, v2, a); a = mfma(sel, v3, a);
          const float mean = row_sum(__uint_as_float(st.x)) * (1.0f / D);
          const float rstd = __builtin_amdgcn_rsqf(fmaxf(fmaf(-mean, mean, row_sum(__uint_as_float(st.y)) * (1.0f / D)), 0.f) + EPS);
          *(LAS unsigned*)(L + lQw) = cvtpk(fmaf(-mean, cs.x, a.x) * rstd, fmaf(-mean, cs.y, a.y) * rstd);
          const unsigned mx = umax4(umax4(umax4(umax4(umax(st.x, st.y), v0), v1), v2), v3);
          if (__all(mx != SENT) || dead) break;
          RETRY(); MISS(6); FENCE(); LD_A();
        }
      } else {
        asm volatile("" : "+v"(pt0), "+v"(pt1), "+v"(pt2), "+v"(pt3), "+v"(sc), "+v"(ls), "+v"(e0), "+v"(e1), "+v"(one8));
      }
      BARRIER();
      STAMP(15);
      ATT_QK(); PT123(); ATT_PV(ATT_L());
    }
    BARRIER();
    STAMP(4 + 12 * Lr);
    unsigned sbv = 0;
    if (Lr == 0) sbv = *(LAS unsigned*)(L + L_QF + 8);
    {
      const f2 ml = *(LAS f2*)(L + lMr);
      float ov[8];
#pragma unroll
      for (int i = 0; i < 8; ++i) ov[i] = *(LAS float*)(L + lOr + i * 1024);
      SCHED();
      float M = ml.x;
      asm("s_nop 1\n\tv_max_f32_dpp %0, %0, %0 quad_perm:[1,0,3,2] row_mask:0xf bank_mask:0xf\n\ts_nop 1\n\t"
          "v_max_f32_dpp %0, %0, %0 quad_perm:[2,3,0,1] row_mask:0xf bank_mask:0xf\n\ts_nop 1\n\t"
          "v_max_f32_dpp %0, %0, %0 row_half_mirror row_mask:0xf bank_mask:0xf" : "+v"(M));
      const float e = __builtin_amdgcn_exp2f(ml.x - M);
      float Ls = e * ml.y, o;
      Ls += dpp<XOR1>(Ls); Ls += dpp<XOR2>(Ls); Ls += dpp<0x141>(Ls);
      {
        unsigned q0, q1, q2, q3;
        asm("s_nop 0\n\tv_readlane_b32 %1, %5, 0\n\tv_readlane_b32 %2, %5, 1\n\tv_readlane_b32 %3, %5, 2\n\tv_readlane_b32 %4, %5, 3\n\t"
            "v_mul_f32 %0, %1, %6\n\tv_readlane_b32 %1, %5, 4\n\tv_fmac_f32 %0, %2, %7\n\tv_readlane_b32 %2, %5, 5\n\t"
            "v_fmac_f32 %0, %3, %8\n\tv_readlane_b32 %3, %5, 6\n\tv_fmac_f32 %0, %4, %9\n\tv_readlane_b32 %4, %5, 7\n\t"
            "v_fmac_f32 %0, %1, %10\n\tv_fmac_f32 %0, %2, %11\n\tv_fmac_f32 %0, %3, %12\n\tv_fmac_f32 %0, %4, %13"
            : "=&v"(o), "=&s"(q0), "=&s"(q1), "=&s"(q2), "=&s"(q3)
            : "v"(e), "v"(ov[0]), "v"(ov[1]), "v"(ov[2]), "v"(ov[3]), "v"(ov[4]), "v"(ov[5]), "v"(ov[6]), "v"(ov[7]));
      }
      o *= __builtin_amdgcn_rcpf(Ls);
      st1(cvtpk(o, dpp<XOR1>(o)), ws, sB, RB(Lr) + m * 512);
      st1(__float_as_uint(M + __builtin_amdgcn_logf(Ls)), ws, sBl, RL(Lr) + m * 128);
    }
    STAMP(5 + 12 * Lr);
    if (Lr == 0) SET_SIDES(__builtin_amdgcn_readfirstlane(sbv));
    if (Lr == 1) RESYNC(SW_B2);
    if (oc) {
      if (Lr == 1) wait_until(O + SW_B2);
      else wait_until(poll4(wsp + WS_BCN + (xcd & 3) * BLK + sd + BSLOT(m) + w * 32, __builtin_amdgcn_readfirstlane(tag), spins, dead) + SB1);
      const int rb = RB(Lr) + w * 2048, rl = RL(Lr) + w * 512;
      v2u le; v4u v0, v1;
#define LD_B() do { le = ld2(ws, vBl, rl); v0 = ld4(ws, vB, rb); v1 = ld4(ws, vB + 256, rb); } while (0)
      LD_B();
      SCHED();
      if (Lr == 0) { LDS_ADD(L_GO); STAMP(14); }
      LANE_IDS();
      int lw = L_UO + SPOS(w, 8 * col + 2 * quad), lr = L_UO + (col & 7) * SPR + quad * 16, lT = TRO(w) + hq * 1024 + L_WO;
      int sC = hq * 8192 + w * 128 + (lane & 31) * 4;
      v4u msk = {sel.x ? (col & 1 ? 0xFFFF0000u : 0xFFFFu) : 0u, sel.y ? (col & 1 ? 0xFFFF0000u : 0xFFFFu) : 0u, sel.z ? (col & 1 ? 0xFFFF0000u : 0xFFFFu) : 0u, sel.w ? (col & 1 ? 0xFFFF0000u : 0xFFFFu) : 0u};
      int sx = (col & 1) ? 0xEEEE : 0x4444;
      f4 acc = Z4;
      v8u wf[4];
#define WF_WO() _Pragma("unroll") for (int k = 0; k < 4; ++k) wf[k] = cat(wfrag(L, lT + k * 8192, 1024), wfrag(L, lT + k * 8192 + 4096, 1024))
      if (Lr == 1) { WF_WO(); }
      asm volatile("" : "+v"(lw), "+v"(lr), "+v"(lT), "+v"(sC), "+v"(msk), "+v"(sx), "+v"(acc));
      SCHED();
      for (;;) {
        float M0 = __uint_as_float(le.x), M1 = __uint_as_float(le.y);
        xq_max2(M0, M1);
        const float x0_ = __builtin_amdgcn_exp2f(__uint_as_float(le.x) - M0), x1_ = __builtin_amdgcn_exp2f(__uint_as_float(le.y) - M1);
        const unsigned y0 = cvtpk(x0_, x0_), y1 = cvtpk(x1_, x1_);
        const f4 a0 = mfma(msk & y0, v0, Z4), a1_ = mfma(msk & y1, v1, Z4);
        const float i0 = __builtin_amdgcn_rcpf(xq_sum(bfhi(y0))), i1 = __builtin_amdgcn_rcpf(xq_sum(bfhi(y1)));
        *(LAS unsigned*)(L + lw) = cvtpk(a0.x * i0, a0.y * i0);
        *(LAS unsigned*)(L + lw + 2 * SPS) = cvtpk(a1_.x * i1, a1_.y * i1);
        const unsigned mx = umax4(umax4(umax(le.x, le.y), v0), v1);
        if (__all(mx != SENT) || dead) break;
        RETRY(); MISS(Lr ? 7 : 2); FENCE(); LD_B();
      }
      if (Lr == 0) {
        asm volatile("s_waitcnt lgkmcnt(0)" ::: "memory");
        LDS_ADD(L_CNT + 12);
        lds_count(L + L_CNT + 12, 8u);
      } else BARRIER();
      STAMP(6 + 12 * Lr);
      {
        v4u u[4];
#pragma unroll
        for (int k = 0; k < 4; ++k) u[k] = *(LAS v4u*)(L + lr + k * SPS);
        if (Lr == 0) { WF_WO(); }
#pragma unroll
        for (int k = 0; k < 4; ++k) acc = smfma(u[k], wf[k], acc, sx);
      }
      st1(cvtpk(acc.x + acc.y, acc.z + acc.w), ws, sC, rC0 + Lr * 27 * BLK);
      STAMP(7 + 12 * Lr);
      if (Lr == 1) RESYNC(SW_C2);
    }
    if (Lr == 0) {
      O = lds_time(L + L_CNT + 24);
      STAMP(25); STAMPV(30, O); Oanch = O;
    }
#define CS_PUB() st2(csr, ws, (col & 3) == 0 ? 2048 + (64 * w + 32 * (col >> 3) + 8 * quad + (col & 4)) * 2 : OOB, WS_A1 + (g * 16 + m) * A1S)
#define D_CONSTS() do { const int sl_ = 8 * w + gc, ga_ = 9 * m + (sl_ >> 1); \
      vD = sl_ < 18 ? (ga_ >> 4) * 3 * BLK + grec * 512 + ((ga_ >> 2) & 3) * 128 + (sl_ & 1) * 64 + (ga_ & 3) * 16 : OOB; vDr = lane < 8 ? vD : OOB; \
      asm volatile("" : "+v"(vD), "+v"(vDr)); } while (0)
    if (mlp) {
      int vc0, vc1;
      {
        const int rc = WS_CD + (Lr * 27 + mi * 3) * BLK + sd;
        v4u v0, v1, v2;
#define LD_C() do { v0 = ld4(ws, vC, rc); v1 = ld4(ws, vC, rc + 4096); if (Lr == 1) v2 = ld4(ws, vCr, rc + 2 * BLK + 4608); } while (0)
        wait_until(O + (Lr ? SW_C2 : SW_C1));
        LD_C();
        SCHED();
        LANE_IDS();
        int lw = L_U1 + SPOS(gb, 16 * w + gn), lr = L_U1 + (col < 10 ? col : col - 8) * SPR + quad * 16, sx = (col & 1) ? 0xEEEE : 0x4444;
        int t1o[3], sP = quad < 2 ? 3 * w * 128 + lane * 4 : OOB, sP2 = quad == 2 && !(col & 1) ? 96 * w + col * 2 : OOB;
#pragma unroll
        for (int k = 0; k < 3; ++k) t1o[k] = TRO(3 * w + k) + hq * 3072 + L_W1 + ((3 * w + k) >> 2) * 1024;
        f4 ar = Z4, a[3] = {Z4, Z4, Z4};
        if (Lr == 0) {
          const unsigned r = *(LAS unsigned*)(L + L_CST + 512 + gb * 128 + (16 * w + gn) * 2);
          ar = f4{0.5f * bf2f(r & 0xFFFFu), 0.5f * bfhi(r), 0.f, 0.f};
          vc0 = gc < 6 ? 8192 + grec * 384 + 96 * w + gc * 16 : OOB; vc1 = lane < 6 ? vc0 : OOB;
          asm volatile("" : "+v"(vc0), "+v"(vc1), "+v"(sP2));
        }
        v8u wf[3];
#define WF_W1() _Pragma("unroll") for (int k = 0; k < 3; ++k) wf[k] = cat(wfrag(L, t1o[k], 3072), wfrag(L, t1o[k] + 12288, 3072))
        if (Lr == 1) { WF_W1(); }
        asm volatile("" : "+v"(lw), "+v"(lr), "+v"(sx), "+v"(sP), "+v"(t1o[0]), "+v"(t1o[1]), "+v"(t1o[2]), "+v"(ar), "+v"(a[0]), "+v"(a[1]), "+v"(a[2]));
          SCHED();
        unsigned xb;
        for (;;) {
          f4 s = mfma(sel3, v0, ar); s = mfma(sel3, v1, s);
          unsigned mx = umax4(umax4(0u, v0), v1);
          if (Lr == 1) { s = mfma(sel3, v2, s); mx = umax4(mx, v2); }
          xb = cvtpk(s.x + dpp<ROR8>(s.x), s.y + dpp<ROR8>(s.y));
          *(LAS unsigned*)(L + lw) = xb;
          if (__all(mx != SENT) || dead) break;
          RETRY(); MISS(Lr ? 8 : 3); FENCE(); LD_C();
        }
        BARRIER();
        STAMP(8 + 12 * Lr);
        const v4u u = *(LAS v4u*)(L + lr);
        if (Lr == 0) { WF_W1(); }
        const int rec = RP(Lr) + pslot;
#pragma unroll
        for (int k = 0; k < 3; ++k) a[k] = smfma(u, wf[k], a[k], sx);
#pragma unroll
        for (int k = 0; k < 3; ++k) st1(cvtpk(a[k].x + a[k].y, a[k].z + a[k].w), ws, sP + 128 * k, rec);
        if (Lr == 0) {
#pragma unroll
          for (int k = 0; k < 3; ++k) { const float cv = a[k].x + a[k].y; st1(cvtpk(cv, dpp<XOR1>(cv)), ws, sP2 + 32 * k, RP(0) + 8192 + mi * 384); }
        }
        STAMP(9 + 12 * Lr);
        if (mf == 0) st1(PAIR_T(xb), ws, grb * 8192 + w * 128 + (gc >> 2) * 64 + (gn + (quad & 1)) * 4, WS_CD + (Lr * 27 + mi * 3 + 2) * BLK + 4096);
        if (Lr == 0) CS_PUB();
        RESYNC(Lr ? SW_P2 : SW_P1);
      }
      {
        const int rp = RP(Lr);
        v4u v[3], t[3], c0, c1; v2u st;
#define LD_P() do { st = ld2(ws, vPs, rp); if (Lr == 0) { c0 = ld4(ws, vc0, rp); c1 = ld4(ws, vc1, rp + 8 * 384); } \
          _Pragma("unroll") for (int k = 0; k < 3; ++k) { v[k] = ld4(ws, vP + 128 * k, rp); t[k] = ld4(ws, vPt + 128 * k, rp + 4 * 1536); } } while (0)
        wait_until(O + (Lr ? SW_P2 : SW_P1));
        LD_P();
        SCHED();
        LANE_IDS();
        int lw[3], lr = L_UH + (col & 7) * SPR + quad * 16, lT = TRO(w) + hq * 1024 + L_W2, sx = (col & 1) ? 0xEEEE : 0x4444, sC = hq * 8192 + w * 128 + (lane & 31) * 4;
        int bp = (((gc >> 1) & 1) + 8 * grb + 16 * (2 * (gc & 1) + hq)) * 4;
        float b1s[3];
#pragma unroll
        for (int k = 0; k < 3; ++k) { lw[k] = L_UH + SPOS(gb, 48 * w + 16 * k + gn) + 2 * grb; b1s[k] = bf2f(*(LAS u16*)(L + L_CST + (48 * w + 16 * k + gn + grb) * 2)); }
        f4 acc = Z4;
        if (mf == 0) { const float bb = bf2f(*(LAS u16*)(L + L_CST + 384 + (16 * w + col) * 2)); acc = f4{bb, 0.f, bb, 0.f}; }
        v8u wf[3];
#define WF_W2() _Pragma("unroll") for (int k = 0; k < 3; ++k) wf[k] = cat(wfrag(L, lT + k * 8192, 1024), wfrag(L, lT + k * 8192 + 4096, 1024))
        if (Lr == 1) { WF_W2(); }
        if (Lr == 0) { D_CONSTS(); asm volatile("" : "+v"(bp)); }
        asm volatile("" : "+v"(lw[0]), "+v"(lw[1]), "+v"(lw[2]), "+v"(lr), "+v"(lT), "+v"(sx), "+v"(sC), "+v"(b1s[0]), "+v"(b1s[1]), "+v"(b1s[2]), "+v"(acc));
        SCHED();
        for (;;) {
          unsigned mx = umax(st.x, st.y);
          float S = __uint_as_float(st.x), Q = __uint_as_float(st.y);
          S += dpp<XOR1>(S); Q += dpp<XOR1>(Q); S += dpp<XOR2>(S); Q += dpp<XOR2>(Q); S += dpp<ROR8>(S); Q += dpp<ROR8>(Q);
          const auto s2 = __builtin_amdgcn_permlane32_swap(__float_as_uint(S), __float_as_uint(S), false, false);
          const auto q2 = __builtin_amdgcn_permlane32_swap(__float_as_uint(Q), __float_as_uint(Q), false, false);
          const float mean = (__uint_as_float(s2[0]) + __uint_as_float(s2[1])) * (1.0f / D);
          const float rstd = __builtin_amdgcn_rsqf(fmaxf(fmaf(-mean, mean, (__uint_as_float(q2[0]) + __uint_as_float(q2[1])) * (1.0f / D)), 0.f) + EPS);
          if (Lr == 0) {
            mx = umax4(umax4(mx, c0), c1);
            f4 a = mfma(sel, c0, Z4); a = mfma(sel, c1, a);
            const float cv = (grb ? a.y : a.x) + dpp<ROR8>(grb ? a.x : a.y);
#pragma unroll
            for (int k = 0; k < 3; ++k) csh[k] = __uint_as_float((unsigned)__builtin_amdgcn_ds_bpermute(bp + 8 * k, (int)__float_as_uint(cv)));
          }
#pragma unroll
          for (int k = 0; k < 3; ++k) {
            f4 a = mfma(sel3, v[k], Z4); a = mfma(sel3, t[k], a);
            const float z = (grb ? a.y : a.x) + dpp<ROR8>(grb ? a.x : a.y);
            const float h = gelu(fmaf(fmaf(-mean, csh[k], z), rstd, b1s[k]));
            *(LAS u16*)(L + lw[k]) = (u16)cvtpk(h, h);
            mx = umax4(umax4(mx, v[k]), t[k]);
          }
          if (__all(mx != SENT) || dead) break;
          RETRY(); MISS(Lr ? 9 : 4); FENCE(); LD_P();
        }
        BARRIER();
        STAMP(10 + 12 * Lr);
        {
          v4u u[3];
#pragma unroll
          for (int k = 0; k < 3; ++k) u[k] = *(LAS v4u*)(L + lr + k * SPS);
          if (Lr == 0) { WF_W2(); }
#pragma unroll
          for (int k = 0; k < 3; ++k) acc = smfma(u[k], wf[k], acc, sx);
        }
        st1(cvtpk(acc.x + acc.y, acc.z + acc.w), ws, sC, rD0 + Lr * 27 * BLK);
        STAMP(11 + 12 * Lr);
        RESYNC(Lr ? SW_D2 : SW_D1);
      }
    } else if (Lr == 0) {
      LANE_IDS();
      CS_PUB();
      D_CONSTS();
    }
    if (Lr == 1) {
      if (oc && g < 4 && w == 0) {
        const int rd = WS_CD + (27 + m * 3 + 1) * BLK + sd + g * 128;
        v4u v0, v1, v2, v3;
#define LD_D(vo, vr, base) do { v0 = ld4(ws, vo, base); v1 = ld4(ws, vo, base + 4096); v2 = ld4(ws, vo, base + BLK); v3 = ld4(ws, vr, base + BLK + 4096); } while (0)
        wait_until(O + SW_D2);
        LD_D(vC, vCr, rd);
        SCHED();
        LANE_IDS();
        const rsrc_t ro = __builtin_amdgcn_make_buffer_rsrc((void*)out, 0, 4 * D * 2, 0x00020000);
        int so = grb ? OOB : (gb * D + 64 * m + 16 * g + gn) * 2;
        asm volatile("" : "+v"(so));
        SCHED();
        unsigned xb;
        for (;;) {
          f4 a = mfma(sel3, v0, Z4); a = mfma(sel3, v1, a); a = mfma(sel3, v2, a); a = mfma(sel3, v3, a);
          xb = cvtpk(a.x + dpp<ROR8>(a.x), a.y + dpp<ROR8>(a.y));
          const unsigned mx = umax4(umax4(umax4(umax4(0u, v0), v1), v2), v3);
          if (__all(mx != SENT) || dead) break;
          RETRY(); MISS(10); FENCE(); LD_D(vC, vCr, rd);
        }
        __builtin_amdgcn_raw_buffer_store_b32(xb, ro, so, 0, OUTAUX);
        if (c == 0) st1(tag, ws, lane == 0 ? 0 : OOB, 0);
        STAMP(24);
        if (LITE) tout = now();
      }
      break;
    }
    {
      v4u v0, v1, v2, v3;
      wait_until(O + SW_D1);
      LD_D(vD, vDr, WS_CD + BLK + sd);
      SCHED();
      LANE_IDS();
      v4u wf[8];
#pragma unroll
      for (int s = 0; s < 4; ++s) { wf[2 * s] = WQ0(s); wf[2 * s + 1] = WQ1(s); }
      const int sl = 8 * w + gc, ga = 9 * m + (sl >> 1);
      int lw = sl < 18 ? L_UX + (2 * (sl & 1) + (quad & 1)) * UXS + (4 * (sl >> 1) + 2 * hq) * 2 : L_DUM, lr = L_UX + (col & 3) * UXS + quad * 16;
      int sA = col < 4 ? col * 512 + (64 * w + 8 * quad) * 2 : OOB;
      vA = quad * 2048 + ab * 512 + (w & 1) * 256 + col * 16; vAs = col * 128 + 8 * ab;
      asm volatile("" : "+v"(lw), "+v"(lr), "+v"(sA), "+v"(vA), "+v"(vAs));
      SCHED();
      unsigned xb;
      for (;;) {
        f4 a = mfma(sel3, v0, Z4); a = mfma(sel3, v1, a); a = mfma(sel3, v2, a); a = mfma(sel3, v3, a);
        xb = cvtpk(a.x + dpp<ROR8>(a.x), a.y + dpp<ROR8>(a.y));
        *(LAS unsigned*)(L + lw) = xb;
        const unsigned mx = umax4(umax4(umax4(umax4(0u, v0), v1), v2), v3);
        if (__all(mx != SENT) || dead) break;
        RETRY(); MISS(5); FENCE(); LD_D(vD, vDr, WS_CD + BLK + sd);
      }
      BARRIER();
      STAMP(12);
      const v4u u0 = *(LAS v4u*)(L + lr), u1 = *(LAS v4u*)(L + lr + 64);
      const int rec = RA2;
      f4 a[4];
#pragma unroll
      for (int s = 0; s < 2; ++s) a[s] = mfma(wf[2 * s], u0, Z4);
#pragma unroll
      for (int s = 0; s < 2; ++s) a[s] = mfma(wf[2 * s + 1], u1, a[s]);
#pragma unroll
      for (int s = 2; s < 4; ++s) a[s] = mfma(wf[2 * s], u0, Z4);
#pragma unroll
      for (int s = 2; s < 4; ++s) a[s] = mfma(wf[2 * s + 1], u1, a[s]);
#pragma unroll
      for (int hp = 0; hp < 2; ++hp) {
        const v2u p0 = pack4(a[2 * hp]), p1 = pack4(a[2 * hp + 1]);
        st4(v4u{p0.x, p0.y, p1.x, p1.y}, ws, sA + 64 * hp, rec);
      }
      STAMP(13);
      if (g == 0) st1(PAIR_T(xb), ws, sl < 18 ? grb * 8192 + (ga >> 4) * 3 * BLK + ((ga >> 2) & 3) * 128 + (sl & 1) * 64 + (4 * (ga & 3) + 2 * hq + (quad & 1)) * 4 : OOB, WS_CD + (27 + 2) * BLK + 4608);
      RESYNC(SW_A2);
    }
  }
  STAMPV(31, spins);
}

extern "C" void kernel_launch(void* const* d_in, const int* in_sizes, int n_in, void* d_out, int out_size, void* d_ws, size_t ws_size,
                              hipStream_t stream) {
  (void)in_sizes; (void)n_in; (void)out_size;
  blk_kernel<<<256, 512, 0, stream>>>((const u16*)d_in[0], (const u16*)d_in[1], (const u16*)d_in[2], (const u16*)d_in[3], (const u16*)d_in[4],
                                      (const u16*)d_in[6], (const u16*)d_in[7], (const u16*)d_in[8], (const u16*)d_in[9], (u16*)d_out,
                                      (unsigned char*)(((size_t)d_ws + ws_size - (4u << 20)) & ~(size_t)((2u << 20) - 1)));
}
```
